```python
import torch

def _softmax(x, axis=-1):
    return torch.softmax(x, dim=axis)
def _gelu(x):
    return torch.nn.functional.gelu(x, approximate="tanh")
def _silu(x):
    return torch.nn.functional.silu(x)
def _sigmoid(x):
    return torch.sigmoid(x)
def _layernorm(x, axis=-1, eps=1e-5):
    m = x.mean(dim=axis, keepdim=True)
    v = x.var(dim=axis, keepdim=True, unbiased=False)
    return (x - m) / torch.sqrt(v + eps)
def _scatter_add(tgt, idx, src, axis=0):
    return tgt.clone().index_add_(axis, idx, src)
def _segment_sum(src, seg, n):
    out = torch.zeros((n,) + src.shape[1:], dtype=src.dtype)
    return out.index_add_(0, seg, src)
def _pad(x, pw):
    flat = tuple(v for pair in reversed(pw) for v in pair)
    return torch.nn.functional.pad(x, flat)

kernel_name = 'standalone__moe_arch_mod__sampling'

def setup_inputs():

    _w = (lambda a: (lambda t: t.to(torch.bfloat16) if t.is_floating_point() else t)(torch.as_tensor(a)))
    B, S, D, F, C = 8, 4, 1024, 3584, 1
    return {
        "x": _w((lambda *s: torch.randn(*s, dtype=torch.bfloat16) * 0.1)(B, S, D)),
        "w_r": _w((lambda *s: torch.randn(*s, dtype=torch.bfloat16) * 0.1)(D)),
        "w_gate": _w((lambda *s: torch.randn(*s, dtype=torch.bfloat16) * 0.1)(D, F)),
        "w_up": _w((lambda *s: torch.randn(*s, dtype=torch.bfloat16) * 0.1)(D, F)),
        "w_down": _w((lambda *s: torch.randn(*s, dtype=torch.bfloat16) * 0.1)(F, D)),
    }

def reference(x, w_r, w_gate, w_up, w_down):
    B, S, D = x.shape
    C = 1
    r = x @ w_r
    idx = torch.argsort(-r, axis=-1)[:, :C]
    idx_e = (lambda x, n, axis: torch.repeat_interleave(x, n, dim=axis))(idx.reshape(B, C, 1), D, axis=2)
    x_sel = (lambda x, i, axis: torch.take_along_dim(x, i, dim=axis))(x, idx_e, axis=1)
    g = _silu(x_sel @ w_gate)
    u = x_sel @ w_up
    y = (g * u) @ w_down
    r_sel = _sigmoid((lambda x, i, axis: torch.take_along_dim(x, i, dim=axis))(r, idx, axis=1))
    y = y * r_sel.reshape(B, C, 1)
    b_off = (torch.arange(B) * S).reshape(B, 1)
    flat_idx = (idx + b_off).reshape(B * C)
    zero = (x * 0.0).reshape(B * S, D)
    upd = _scatter_add(zero, flat_idx, y.reshape(B * C, D), axis=0)
    return x + upd.reshape(B, S, D)
```

```cpp
#include <hip/hip_runtime.h>
#include <hip/hip_bf16.h>
#include <stdint.h>

namespace {
constexpr int B = 8, S = 4, D = 1024, F = 3584;
typedef unsigned short u16;

__device__ __forceinline__ float bf2f(u16 v) { return __uint_as_float(((unsigned)v) << 16); }
__device__ __forceinline__ u16 f2bf(float f) { return __builtin_bit_cast(u16, (__bf16)f); }
__device__ __forceinline__ float rbf(float f) { return bf2f(f2bf(f)); }

__device__ void router(const u16* x, const u16* wr, float* sr, int* sidx, float* ssig) {
  const int tid = threadIdx.x, lane = tid & 63, w = tid >> 6;
  for (int t = w; t < B * S; t += blockDim.x / 64) {
    float a = 0.f;
    for (int k = lane; k < D; k += 64) a += bf2f(x[t * D + k]) * bf2f(wr[k]);
    for (int o = 32; o; o >>= 1) a += __shfl_xor(a, o);
    if (lane == 0) sr[t] = rbf(a);
  }
  __syncthreads();
  if (tid < B) {
    int best = 0; float bv = sr[tid * S];
    for (int s = 1; s < S; ++s) { float v = sr[tid * S + s]; if (v > bv || (bv != bv && v == v)) { bv = v; best = s; } }
    sidx[tid] = best;
    ssig[tid] = rbf(1.f / (1.f + __expf(-bv)));
  }
  __syncthreads();
}

__global__ void k1(const u16* x, const u16* wr, const u16* wg, const u16* wu, u16* h) {
  __shared__ float sr[32]; __shared__ int sidx[8]; __shared__ float ssig[8];
  __shared__ float xs[8][D];
  __shared__ float red[16][2][8][16];
  router(x, wr, sr, sidx, ssig);
  for (int i = threadIdx.x; i < 8 * D; i += blockDim.x) { int b = i / D, k = i % D; xs[b][k] = bf2f(x[(b * S + sidx[b]) * D + k]); }
  __syncthreads();
  const int c = threadIdx.x & 15, kg = threadIdx.x >> 4;
  const int col = blockIdx.x * 16 + c;
  float ag[8] = {0}, au[8] = {0};
  for (int k = kg; k < D; k += 16) {
    float g = bf2f(wg[(size_t)k * F + col]), u = bf2f(wu[(size_t)k * F + col]);
#pragma unroll
    for (int b = 0; b < 8; ++b) { ag[b] += xs[b][k] * g; au[b] += xs[b][k] * u; }
  }
#pragma unroll
  for (int b = 0; b < 8; ++b) { red[kg][0][b][c] = ag[b]; red[kg][1][b][c] = au[b]; }
  __syncthreads();
  if (threadIdx.x < 128) {
    int b = threadIdx.x >> 4;
    float g = 0, u = 0;
    for (int j = 0; j < 16; ++j) { g += red[j][0][b][c]; u += red[j][1][b][c]; }
    g = rbf(g); u = rbf(u);
    float sg = rbf(g / (1.f + __expf(-g)));
    h[b * F + col] = f2bf(sg * u);
  }
}

__global__ void k2(const u16* h, const u16* wd, float* part) {
  __shared__ float hs[8][224];
  __shared__ float red[4][8][64];
  const int ct = blockIdx.x & 15, ks = blockIdx.x >> 4;
  for (int i = threadIdx.x; i < 8 * 224; i += blockDim.x) { int b = i / 224, k = i % 224; hs[b][k] = bf2f(h[b * F + ks * 224 + k]); }
  __syncthreads();
  const int c = threadIdx.x & 63, kg = threadIdx.x >> 6;
  float a[8] = {0};
  for (int k = kg; k < 224; k += 4) {
    float w = bf2f(wd[(size_t)(ks * 224 + k) * D + ct * 64 + c]);
#pragma unroll
    for (int b = 0; b < 8; ++b) a[b] += hs[b][k] * w;
  }
#pragma unroll
  for (int b = 0; b < 8; ++b) red[kg][b][c] = a[b];
  __syncthreads();
  for (int i = threadIdx.x; i < 512; i += blockDim.x) {
    int b = i >> 6, cc = i & 63;
    part[(ks * 8 + b) * D + ct * 64 + cc] = red[0][b][cc] + red[1][b][cc] + red[2][b][cc] + red[3][b][cc];
  }
}

__global__ void k3(const u16* x, const u16* wr, const float* part, u16* out) {
  __shared__ float sr[32]; __shared__ int sidx[8]; __shared__ float ssig[8];
  router(x, wr, sr, sidx, ssig);
  const int t = blockIdx.x, b = t / S, s = t % S;
  for (int d = threadIdx.x; d < D; d += blockDim.x) {
    float xv = bf2f(x[t * D + d]);
    float upd = rbf(xv * 0.0f);
    if (s == sidx[b]) {
      float y = 0;
      for (int j = 0; j < 16; ++j) y += part[(j * 8 + b) * D + d];
      y = rbf(rbf(y) * ssig[b]);
      upd = rbf(upd + y);
    }
    out[t * D + d] = f2bf(xv + upd);
  }
}
}

extern "C" void kernel_launch(void* const* d_in, const int* in_sizes, int n_in,
                              void* d_out, int out_size, void* d_ws, size_t ws_size,
                              hipStream_t stream) {
  const u16* x = (const u16*)d_in[0];
  const u16* wr = (const u16*)d_in[1];
  const u16* wg = (const u16*)d_in[2];
  const u16* wu = (const u16*)d_in[3];
  const u16* wd = (const u16*)d_in[4];
  u16* h = (u16*)d_ws;
  float* part = (float*)((char*)d_ws + 65536);
  k1<<<224, 256, 0, stream>>>(x, wr, wg, wu, h);
  k2<<<256, 256, 0, stream>>>(h, wd, part);
  k3<<<32, 256, 0, stream>>>(x, wr, part, (u16*)d_out);
}
```

```cpp
#include <hip/hip_runtime.h>
#include <stdint.h>

namespace {
__global__ void __launch_bounds__(512, 2) mod_ffn(const void* x, const void* wr, const void* wg, const void* wu, const void* wd, void* out, void* ws) {
  __shared__ __attribute__((aligned(1024))) char lds_[139264];
  asm volatile(
    "s_load_dwordx2 s[20:21], %[ka], 0x0\n"
    "s_xor_b32 s3, %[bid], 2\n"
    "s_and_b32 s48, s3, 8\n"
    "s_or_b32 s48, s48, 16\n"
    "s_and_b32 s49, s3, 0xe7\n"
    "s_cselect_b32 s48, s48, 8\n"
    "s_load_dwordx2 s[24:25], %[ka], s48\n"
    "v_readfirstlane_b32 s17, %[tid]\n"
    "s_load_dwordx2 s[36:37], %[ka], 0x30\n"
    "s_load_dwordx2 s[28:29], %[ka], 0x20\n"
    "s_load_dwordx2 s[40:41], %[ka], 0x28\n"
    "s_mov_b32 s27, 0x20000\n"
    "s_mov_b32 s22, 0x10000\n"
    "s_mov_b32 s23, s27\n"
    "s_cbranch_scc0 .Lrouter\n"
    "s_lshr_b32 s47, s3, 3\n"
    "s_and_b32 s18, s3, 7\n"
    "s_mul_i32 s19, s18, 28\n"
    "s_add_i32 s19, s19, s47\n"
    "s_add_i32 s19, s19, -4\n"
    "s_and_b32 s49, s47, 2\n"
    "s_add_i32 s49, s49, s47\n"
    "s_lshl_b32 s50, s18, 5\n"
    "s_add_i32 s49, s49, s50\n"
    "s_addk_i32 s49, -22\n"
    "s_cmp_lt_u32 s47, 4\n"
    "s_cselect_b32 s49, s49, s19\n"
    "s_cselect_b32 s50, 1, 0\n"
    "s_bfe_u32 s53, s49, 0x10002\n"
    "s_lshl1_add_u32 s53, s53, s50\n"
    "s_and_b32 s50, s49, 0x1a\n"
    "s_cmp_eq_u32 s50, 10\n"
    "s_cselect_b32 s34, 0xc000, 0\n"
    "s_cmpk_ge_u32 s17, 0x100\n"
    "s_cbranch_scc1 .Ldispatch\n"
    "s_setprio 1\n"
    "v_lshrrev_b32 v2, 3, %[tid]\n"
    "v_lshrrev_b32 v3, 4, %[tid]\n"
    "s_lshr_b32 s50, s49, 3\n"
    "s_bfe_u32 s51, s49, 0x10001\n"
    "v_and_b32 v5, 2, v2\n"
    "s_lshl1_add_u32 s50, s50, s51\n"
    "v_and_or_b32 v5, v3, 4, v5\n"
    "s_lshl_b32 s50, s50, 7\n"
    "v_bitop3_b32 v5, %[tid], v5, 7 bitop3:0x28\n"
    "s_mul_i32 s49, s53, 0x1c0000\n"
    "v_mul_u32_u24 v6, 0x1c0, v2\n"
    "s_add_u32 s49, s49, s50\n"
    "v_add_lshl_u32 v5, v6, v5, 4\n"
    "s_lshl4_add_u32 m0, s17, s34\n"
    "s_addk_i32 m0, 8192\n"
    "s_mov_b32 s26, 0x700000\n"
    "s_mov_b32 s51, 0x38000\n"
    "v_bitop3_b32 v4, %[tid], v3, 7 bitop3:0x28\n"
    "s_lshl_b32 s48, s53, 9\n"
    "v_lshlrev_b32 v4, 4, v4\n"
    "v_lshl_or_b32 v4, v2, 11, v4\n"
    "s_waitcnt lgkmcnt(0)\n"
    "buffer_load_dwordx4 v5, s[24:27], s49 offen nt lds\n"
    "s_addk_i32 m0, 8192\n"
    "s_add_u32 s49, s49, s51\n"
    "buffer_load_dwordx4 v5, s[24:27], s49 offen nt lds\n"
    "s_addk_i32 m0, -16384\n"
    "s_add_u32 s49, s49, s51\n"
    "buffer_load_dwordx4 v4, s[20:23], s48 offen lds\n"
    "s_addk_i32 m0, 12288\n"
    "s_addk_i32 s48, 0x80\n"
    "buffer_load_dwordx4 v4, s[20:23], s48 offen lds\n"
    "s_addk_i32 m0, 8192\n"
    "s_addk_i32 s48, 0x80\n"
    "s_sleep 9\n"
    "buffer_load_dwordx4 v5, s[24:27], s49 offen nt lds\n"
    "s_addk_i32 m0, 4096\n"
    "s_add_u32 s49, s49, s51\n"
    "buffer_load_dwordx4 v4, s[20:23], s48 offen lds\n"
    "s_addk_i32 m0, 4096\n"
    "s_addk_i32 s48, 0x80\n"
    "buffer_load_dwordx4 v5, s[24:27], s49 offen nt lds\n"
    "s_addk_i32 m0, 4096\n"
    "s_add_u32 s49, s49, s51\n"
    "buffer_load_dwordx4 v5, s[24:27], s49 offen nt lds\n"
    "s_addk_i32 m0, 4096\n"
    "s_add_u32 s49, s49, s51\n"
    "s_branch .Lsetup\n"
    ".Ldispatch:\n"
    "s_cmpk_eq_u32 s17, 0x180\n"
    "s_cbranch_scc1 .Lpad1\n"
    "s_cmpk_eq_u32 s17, 0x1c0\n"
    "s_cbranch_scc1 .Lpad2\n"
    "s_branch .Lcompute\n"
    ".Lrouter:\n"
    "s_lshr_b32 s47, s3, 3\n"
    "s_lshr_b32 s17, s17, 6\n"
    "v_and_b32 v1, 63, %[tid]\n"
    "s_lshr_b32 s48, s17, 2\n"
    "s_lshl2_add_u32 s49, s48, s47\n"
    "s_and_b32 s50, s17, 3\n"
    "s_lshl2_add_u32 s49, s49, s50\n"
    "s_lshl_b32 s49, s49, 11\n"
    "s_movk_i32 s26, 0x800\n"
    "v_lshlrev_b32 v2, 4, v1\n"
    "s_mov_b32 s38, 1572864\n"
    "s_mov_b32 s39, s27\n"
    "s_mul_i32 s12, s47, 0x2080\n"
    "s_bitset0_b32 s12, 14\n"
    "s_mov_b32 s13, 0\n"
    "s_cmp_lg_u32 s17, 0\n"
    "s_waitcnt lgkmcnt(0)\n"
    "s_cbranch_scc1 .Lrl\n"
    "s_mov_b64 exec, 0xff\n"
    "buffer_store_dwordx4 v[4:7], v2, s[36:39], s12 offen offset:1024 sc1\n"
    "s_mov_b64 exec, -1\n"
    ".Lrl:\n"
    "s_barrier\n"
    "buffer_load_dwordx4 v[4:7], v2, s[20:23], s49 offen\n"
    "buffer_load_dwordx4 v[12:15], v2, s[24:27], 0 offen\n"
    "buffer_load_dwordx4 v[8:11], v2, s[20:23], s49 offen offset:1024\n"
    "buffer_load_dwordx4 v[16:19], v2, s[24:27], 0 offen offset:1024\n"
    "s_load_dword s45, s[36:37], 0x0\n"
    "s_mov_b32 s42, s22\n"
    "s_mov_b32 s43, s27\n"
    "v_mov_b32 v20, 0\n"
    "s_lshl_b32 s51, s17, 2\n"
    "v_mov_b32 v21, s51\n"
    "v_and_b32 v22, 16, v2\n"
    "s_lshl_b32 s51, s47, 7\n"
    "s_addk_i32 s51, 4096\n"
    "v_lshlrev_b32 v23, 3, v1\n"
    "v_and_b32 v24, 48, v1\n"
    "v_mad_u32_u24 v23, v24, 24, v23\n"
    ".Lrc:\n"
    "s_add_u32 s13, s13, 4\n"
    "s_getreg_b32 s12, hwreg(HW_REG_IB_STS, 0, 4)\n"
    "s_cmp_gt_u32 s12, 4\n"
    "s_cbranch_scc1 .Lrc\n"
    "s_min_u32 s13, s13, 0x7ffc\n"
    "s_waitcnt vmcnt(2)\n"
    "v_dot2c_f32_bf16 v20, v4, v12\n"
    "v_dot2c_f32_bf16 v20, v5, v13\n"
    "v_dot2c_f32_bf16 v20, v6, v14\n"
    "v_dot2c_f32_bf16 v20, v7, v15\n"
    "s_waitcnt vmcnt(0)\n"
    "v_dot2c_f32_bf16 v20, v8, v16\n"
    "v_dot2c_f32_bf16 v20, v9, v17\n"
    "v_dot2c_f32_bf16 v20, v10, v18\n"
    "v_dot2c_f32_bf16 v20, v11, v19\n"
    "s_nop 2\n"
    "v_add_f32_dpp v20, v20, v20 quad_perm:[1,0,3,2] row_mask:0xf bank_mask:0xf\n"
    "s_nop 1\n"
    "v_add_f32_dpp v20, v20, v20 quad_perm:[2,3,0,1] row_mask:0xf bank_mask:0xf\n"
    "s_nop 1\n"
    "v_add_f32_dpp v20, v20, v20 row_ror:4 row_mask:0xf bank_mask:0xf\n"
    "s_nop 1\n"
    "v_add_f32_dpp v20, v20, v20 row_ror:8 row_mask:0xf bank_mask:0xf\n"
    "s_nop 1\n"
    "v_add_f32_dpp v20, v20, v20 row_bcast:15 row_mask:0xa bank_mask:0xf\n"
    "s_nop 1\n"
    "v_add_f32_dpp v20, v20, v20 row_bcast:31 row_mask:0xc bank_mask:0xf\n"
    "s_mov_b32 exec_lo, 0\n"
    "s_brev_b32 exec_hi, 1\n"
    "v_cvt_pk_bf16_f32 v20, 0, v20\n"
    "ds_write_b32 v21, v20\n"
    "s_mov_b64 exec, -1\n"
    "s_waitcnt lgkmcnt(0)\n"
    "s_barrier\n"
    "ds_read_b128 v[24:27], v22\n"
    "s_add_i32 s45, s45, 0x1010101\n"
    "v_mov_b32 v31, s45\n"
    "s_cmp_eq_u32 s17, 0\n"
    "s_cbranch_scc1 .Lr0\n"
    "s_sleep 2\n"
    ".Lr0:\n"
    "s_waitcnt lgkmcnt(0)\n"
    "v_max3_f32 v29, v24, v25, v26\n"
    "v_max_f32 v29, v29, v27\n"
    "v_cmp_eq_f32 s[4:5], v27, v29\n"
    "v_mul_f32 v32, 0xbfb8aa3b, v29\n"
    "v_cmp_eq_f32 s[6:7], v26, v29\n"
    "v_exp_f32 v32, v32\n"
    "v_cmp_eq_f32 s[8:9], v25, v29\n"
    "v_add_f32 v32, 1.0, v32\n"
    "v_cmp_eq_f32 vcc, v24, v29\n"
    "v_rcp_f32 v32, v32\n"
    "v_cndmask_b32 v28, 0, 3, s[4:5]\n"
    "v_cndmask_b32 v28, v28, 2, s[6:7]\n"
    "v_cndmask_b32 v28, v28, 1, s[8:9]\n"
    "v_cndmask_b32 v28, v28, 0, vcc\n"
    "v_cvt_pk_bf16_f32 v32, 0, v32\n"
    "v_or3_b32 v30, v32, v28, s13\n"
    "s_cbranch_scc0 .Lr1\n"
    "buffer_store_dwordx2 v[30:31], v23, s[36:39], s51 offen sc1\n"
    "buffer_store_dwordx2 v[30:31], v23, s[36:39], s51 offen offset:2048 sc1\n"
    ".Lr1:\n"
    "v_readlane_b32 s52, v28, s48\n"
    "s_mov_b32 s12, 0xffff0000\n"
    "s_cmp_eq_u32 s52, s50\n"
    "s_cbranch_scc1 .Lr3\n"
    "s_mov_b32 s51, 1\n"
    ".Lr2:\n"
    "v_lshlrev_b32 v12, 16, v4\n"
    "v_and_b32 v13, s12, v4\n"
    "v_fmac_f32 v12, 0, v12\n"
    "v_fmac_f32 v13, 0, v13\n"
    "v_cvt_pk_bf16_f32 v16, v12, v13\n"
    "v_mov_b32 v4, v8\n"
    "v_lshlrev_b32 v12, 16, v5\n"
    "v_and_b32 v13, s12, v5\n"
    "v_fmac_f32 v12, 0, v12\n"
    "v_fmac_f32 v13, 0, v13\n"
    "v_cvt_pk_bf16_f32 v17, v12, v13\n"
    "v_mov_b32 v5, v9\n"
    "v_lshlrev_b32 v12, 16, v6\n"
    "v_and_b32 v13, s12, v6\n"
    "v_fmac_f32 v12, 0, v12\n"
    "v_fmac_f32 v13, 0, v13\n"
    "v_cvt_pk_bf16_f32 v18, v12, v13\n"
    "v_mov_b32 v6, v10\n"
    "v_lshlrev_b32 v12, 16, v7\n"
    "v_and_b32 v13, s12, v7\n"
    "v_fmac_f32 v12, 0, v12\n"
    "v_fmac_f32 v13, 0, v13\n"
    "v_cvt_pk_bf16_f32 v19, v12, v13\n"
    "v_mov_b32 v7, v11\n"
    "buffer_store_dwordx4 v[16:19], v2, s[40:43], s49 offen\n"
    "s_addk_i32 s49, 0x400\n"
    "s_sub_u32 s51, s51, 1\n"
    "s_cbranch_scc0 .Lr2\n"
    ".Lr3:\n"
    "s_endpgm\n"
    ".Lback_loader:\n"
    "v_and_b32 v6, 6, v2\n"
    "v_and_or_b32 v6, v3, 8, v6\n"
    "v_bitop3_b32 v6, %[tid], v6, 15 bitop3:0x28\n"
    "v_lshlrev_b32 v6, 4, v6\n"
    "v_lshl_or_b32 v6, v3, 11, v6\n"
    "s_and_b32 s53, s19, 7\n"
    "s_lshl_b32 s50, s44, 10\n"
    "s_or_b32 s50, s50, s53\n"
    "s_lshl_b32 s50, s50, 8\n"
    "s_cmp_lt_u32 s47, 4\n"
    "s_cselect_b32 s30, 0, s26\n"
    "s_mov_b32 s31, s27\n"
    "s_lshr_b32 s52, s22, 1\n"
    "s_mov_b32 s16, 3\n"
    "s_waitcnt vmcnt(7)\n"
    "buffer_load_dwordx4 v4, s[20:23], s48 offen lds\n"
    "s_addk_i32 m0, 4096\n"
    "s_addk_i32 s48, 0x80\n"
    "s_cmp_lg_u32 s34, 0\n"
    "s_cbranch_scc1 .Lshort\n"
    ".Lstream:\n"
    "s_waitcnt vmcnt(7)\n"
    "buffer_load_dwordx4 v5, s[24:27], s49 offen nt lds\n"
    "s_addk_i32 m0, 4096\n"
    "s_add_u32 s49, s49, s51\n"
    "s_waitcnt vmcnt(7)\n"
    "buffer_load_dwordx4 v5, s[24:27], s49 offen nt lds\n"
    "s_addk_i32 m0, 4096\n"
    "s_add_u32 s49, s49, s51\n"
    "s_waitcnt vmcnt(7)\n"
    "s_barrier\n"
    "buffer_load_dwordx4 v4, s[20:23], s48 offen lds\n"
    "s_addk_i32 m0, 4096\n"
    "s_addk_i32 s48, 0x80\n"
    "s_sub_u32 s16, s16, 1\n"
    "s_cbranch_scc0 .Lstream\n"
    ".Lshort:\n"
    "s_waitcnt vmcnt(7)\n"
    "buffer_load_dwordx4 v5, s[24:27], s49 offen nt lds\n"
    "s_addk_i32 m0, 4096\n"
    "s_add_u32 s49, s49, s51\n"
    "s_waitcnt vmcnt(7)\n"
    "buffer_load_dwordx4 v5, s[24:27], s49 offen nt lds\n"
    "s_addk_i32 m0, 8192\n"
    "s_add_u32 s49, s49, s51\n"
    "s_waitcnt vmcnt(7)\n"
    "s_barrier\n"
    "buffer_load_dwordx4 v5, s[24:27], s49 offen nt lds\n"
    "s_addk_i32 m0, 4096\n"
    "s_add_u32 s49, s49, s51\n"
    "s_waitcnt vmcnt(6)\n"
    "buffer_load_dwordx4 v6, s[28:31], s50 offen nt lds\n"
    "s_addk_i32 m0, 4096\n"
    "s_add_u32 s50, s50, s52\n"
    "s_waitcnt vmcnt(6)\n"
    "s_barrier\n"
    "buffer_load_dwordx4 v6, s[28:31], s50 offen nt lds\n"
    "s_addk_i32 m0, 4096\n"
    "s_add_u32 s50, s50, s52\n"
    "s_waitcnt vmcnt(6)\n"
    "buffer_load_dwordx4 v6, s[28:31], s50 offen nt lds\n"
    "s_addk_i32 m0, 4096\n"
    "s_add_u32 s50, s50, s52\n"
    "s_waitcnt vmcnt(6)\n"
    "buffer_load_dwordx4 v6, s[28:31], s50 offen nt lds\n"
    "s_addk_i32 m0, 4096\n"
    "s_add_u32 s50, s50, s52\n"
    "s_waitcnt vmcnt(6)\n"
    "s_barrier\n"
    "s_waitcnt vmcnt(4) lgkmcnt(0)\n"
    "buffer_load_dwordx4 v6, s[28:31], s50 offen nt lds\n"
    "s_addk_i32 m0, 4096\n"
    "s_add_u32 s50, s50, s52\n"
    "buffer_load_dwordx4 v6, s[28:31], s50 offen nt lds\n"
    "s_addk_i32 m0, 4096\n"
    "s_add_u32 s50, s50, s52\n"
    "s_barrier\n"
    "buffer_load_dwordx4 v6, s[28:31], s50 offen nt lds\n"
    "s_addk_i32 m0, 4096\n"
    "s_add_u32 s50, s50, s52\n"
    "buffer_load_dwordx4 v6, s[28:31], s50 offen nt lds\n"
    "s_barrier\n"
    "ds_read_b128 v[76:79], v72 offset:6400\n"
    "s_setprio 0\n"
    "s_add_i32 s45, s45, 0x1010101\n"
    "v_lshrrev_b32 v74, v74, s45\n"
    "v_lshrrev_b32 v75, 8, v74\n"
    "s_waitcnt lgkmcnt(0)\n"
    "v_add_u16 v76, v76, v78\n"
    "v_add_u16 v77, v77, v79\n"
    "v_cmp_gt_u16 vcc, v76, v77\n"
    "s_and_b32 s14, vcc_lo, 0x2000\n"
    "s_branch .Lphase2\n"
    ".Lcompute:\n"
    "s_waitcnt lgkmcnt(0)\n"
    ".Lsetup:\n"
    "s_load_dword s45, s[36:37], 0x0\n"
    "s_lshr_b32 s17, s17, 6\n"
    "v_and_b32 v1, 63, %[tid]\n"
    "v_mov_b32 v72, 0\n"
    "s_mov_b32 s38, 1572864\n"
    "s_mov_b32 s39, s27\n"
    "s_mov_b32 s42, s22\n"
    "s_mov_b32 s43, s27\n"
    "s_lshr_b32 s35, s18, 2\n"
    "s_lshr_b32 s53, s19, 3\n"
    "s_bitcmp1_b32 s18, 1\n"
    "s_cselect_b32 s44, -7, 7\n"
    "s_add_i32 s44, s44, s53\n"
    "v_lshrrev_b32 v61, 4, v1\n"
    "v_and_b32 v62, 15, v1\n"
    "v_lshrrev_b32 v8, 5, v1\n"
    "v_and_b32 v9, 31, v1\n"
    "v_lshlrev_b32 v64, 4, v9\n"
    "v_lshl_or_b32 v64, v8, 11, v64\n"
    "s_and_b32 s59, s17, 3\n"
    "s_lshr_b32 s58, s17, 2\n"
    "s_lshl3_add_u32 s54, s44, s58\n"
    "s_and_b32 s53, s54, 31\n"
    "s_mul_i32 s15, s35, 110\n"
    "s_sub_i32 s54, s54, s15\n"
    "s_lshl3_add_u32 s54, s54, s59\n"
    "s_lshl_b32 s54, s54, 9\n"
    "s_addk_i32 s54, 16384\n"
    "s_lshr_b32 s15, s44, 2\n"
    "s_lshl4_add_u32 s15, s15, s59\n"
    "s_addk_i32 s15, 928\n"
    "s_lshl_b32 s15, s15, 9\n"
    "s_cmp_eq_u32 s53, 9\n"
    "s_cselect_b32 s15, s15, -1\n"
    "v_lshrrev_b32 v10, 2, v1\n"
    "v_and_b32 v11, 3, v1\n"
    "v_lshlrev_b32 v11, 3, v11\n"
    "v_lshrrev_b32 v63, 2, v62\n"
    "v_lshl_or_b32 v63, v61, 3, v63\n"
    "v_bitop3_b32 v12, v10, s17, 7 bitop3:0x28\n"
    "v_lshl_or_b32 v12, v12, 5, v11\n"
    "v_lshl_or_b32 v65, v63, 8, v12\n"
    "v_add_u32 v65, 106496, v65\n"
    "s_mul_i32 s53, s59, 288\n"
    "s_lshl_b32 s55, s58, 7\n"
    "s_add_i32 s53, s53, s55\n"
    "v_mul_u32_u24 v66, 1152, v8\n"
    "v_lshl_add_u32 v66, v9, 2, v66\n"
    "v_add_u32 v66, s53, v66\n"
    "v_lshrrev_b32 v12, 1, v62\n"
    "v_and_b32 v12, 6, v12\n"
    "v_and_or_b32 v12, v1, 1, v12\n"
    "v_mul_u32_u24 v67, 288, v12\n"
    "v_lshl_add_u32 v67, v61, 4, v67\n"
    "s_add_i32 s56, s47, -4\n"
    "s_mul_i32 s53, s56, 37\n"
    "s_lshr_b32 s53, s53, 8\n"
    "s_mul_i32 s55, s53, 7\n"
    "s_sub_i32 s57, s56, s55\n"
    "s_mul_i32 s57, s57, s58\n"
    "s_or_b32 s57, s57, s59\n"
    "s_addk_i32 s53, 28\n"
    "s_cmp_eq_u32 s58, 0\n"
    "s_cselect_b32 s55, s56, s53\n"
    "s_lshl_b32 s53, s18, 2\n"
    "s_lshr_b32 s53, 0x76325410, s53\n"
    "s_and_b32 s53, s53, 7\n"
    "s_lshl_b32 s53, s53, 5\n"
    "s_add_i32 s55, s55, s53\n"
    "s_and_b32 s53, s55, 3\n"
    "v_lshl_or_b32 v13, s53, 1, v8\n"
    "v_lshlrev_b32 v68, 2, v13\n"
    "v_and_b32 v16, 7, v1\n"
    "v_lshlrev_b32 v15, 1, v16\n"
    "v_and_or_b32 v15, v61, 1, v15\n"
    "v_lshlrev_b32 v69, 13, v13\n"
    "v_lshl_or_b32 v69, v15, 1, v69\n"
    "s_andn2_b32 s56, s55, 3\n"
    "s_lshl_b32 s56, s56, 3\n"
    "v_lshlrev_b32 v70, 3, v1\n"
    "v_and_b32 v74, 16, v64\n"
    "s_and_b32 s58, s19, 7\n"
    "s_lshl3_add_u32 s58, s44, s58\n"
    "s_lshl3_add_u32 s58, s58, s17\n"
    "s_addk_i32 s58, 1152\n"
    "s_lshl_b32 s58, s58, 9\n"
    "v_lshrrev_b32 v14, 3, v1\n"
    "v_lshlrev_b32 v13, 15, v14\n"
    "v_lshl_or_b32 v71, v16, 4, v13\n"
    "s_addk_i32 s55, 4608\n"
    "s_lshl_b32 s55, s55, 7\n"
    "s_cmp_lt_u32 s17, 4\n"
    "s_cbranch_scc1 .Lback_loader\n"
    "v_lshlrev_b32 v56, 3, v62\n"
    "v_lshl_or_b32 v56, v61, 9, v56\n"
    "s_andn2_b32 s48, s19, 3\n"
    "s_add_i32 s48, s48, s19\n"
    "s_mul_i32 s53, s35, 220\n"
    "s_sub_i32 s48, s48, s53\n"
    "s_lshl2_add_u32 s53, s18, s47\n"
    "s_addk_i32 s53, 220\n"
    "s_cmp_lt_u32 s47, 4\n"
    "s_cselect_b32 s48, s53, s48\n"
    "s_cselect_b32 s13, 0, -1\n"
    "s_lshl4_add_u32 s48, s48, s59\n"
    "s_lshl_b32 s48, s48, 7\n"
    "s_addk_i32 s48, 16384\n"
    "v_lshrrev_b32 v4, 1, v62\n"
    "v_xor_b32 v4, v61, v4\n"
    "v_lshlrev_b32 v4, 4, v4\n"
    "v_lshl_or_b32 v52, v62, 7, v4\n"
    "v_add_u32 v52, s34, v52\n"
    "v_xor_b32 v53, 64, v52\n"
    "v_bitop3_b32 v4, v14, s59, 3 bitop3:0x28\n"
    "v_lshl_or_b32 v4, v4, 5, v11\n"
    "v_lshl_or_b32 v54, v63, 7, v4\n"
    "v_add_u32 v54, s34, v54\n"
    "v_lshlrev_b32 v57, 7, v61\n"
    "v_and_b32 v12, 1, v1\n"
    "v_lshrrev_b32 v4, 2, v62\n"
    "v_lshlrev_b32 v58, 7, v4\n"
    "v_lshl_or_b32 v58, v12, 3, v58\n"
    "v_lshl_or_b32 v4, v12, 2, v4\n"
    "v_lshlrev_b32 v55, 2, v4\n"
    "v_lshlrev_b32 v60, 2, v61\n"
    "v_add_u32 v60, 6400, v60\n"
    "v_mov_b64 v[32:33], 0\n"
    "v_mov_b64 v[34:35], 0\n"
    "v_mov_b64 v[36:37], 0\n"
    "v_mov_b64 v[38:39], 0\n"
    "v_mov_b32 v73, 86016\n"
    "s_add_i32 s52, s18, 8\n"
    "s_lshl_b32 s52, s52, 9\n"
    "s_cmp_lg_u32 s34, 0\n"
    "s_cselect_b32 s50, 4, 0\n"
    "s_cselect_b32 s49, 9, 5\n"
    "s_movk_i32 s11, 0x3000\n"
    "s_movk_i32 s51, 0xff\n"
    ".Lloop:\n"
    "s_barrier\n"
    "s_waitcnt lgkmcnt(0)\n"
    "ds_read_b64_tr_b16 v[4:5], v54 offset:8192\n"
    "ds_read_b64_tr_b16 v[6:7], v54 offset:8704\n"
    "ds_read_b128 v[12:15], v52\n"
    "ds_read_b128 v[16:19], v52 offset:2048\n"
    "ds_read_b64_tr_b16 v[8:9], v54 offset:16384\n"
    "ds_read_b64_tr_b16 v[10:11], v54 offset:16896\n"
    "ds_read_b128 v[20:23], v53\n"
    "ds_read_b128 v[24:27], v53 offset:2048\n"
    "s_add_i32 s50, s50, 1\n"
    "s_cmp_lg_u32 s50, s49\n"
    "s_cbranch_scc1 .Lnopoll\n"
    "buffer_load_dwordx4 v[28:31], v57, s[36:39], s52 offen sc1\n"
    "buffer_load_dwordx2 v[44:45], v58, s[36:39], s52 offen sc1\n"
    ".Lnopoll:\n"
    "s_cmp_lt_u32 s50, 7\n"
    "v_add_u32 v52, s11, v52\n"
    "v_add_u32 v53, s11, v53\n"
    "v_add_u32 v54, s11, v54\n"
    "s_waitcnt lgkmcnt(5)\n"
    "v_mfma_f32_16x16x32_bf16 v[32:35], v[12:15], v[4:7], v[32:35]\n"
    "s_waitcnt lgkmcnt(4)\n"
    "v_mfma_f32_16x16x32_bf16 v[36:39], v[16:19], v[4:7], v[36:39]\n"
    "s_waitcnt lgkmcnt(1)\n"
    "v_mfma_f32_16x16x32_bf16 v[32:35], v[20:23], v[8:11], v[32:35]\n"
    "s_waitcnt lgkmcnt(0)\n"
    "v_mfma_f32_16x16x32_bf16 v[36:39], v[24:27], v[8:11], v[36:39]\n"
    "s_cbranch_scc1 .Lloop\n"
    "s_add_i32 s45, s45, 0x1010101\n"
    "v_lshrrev_b32 v74, v74, s45\n"
    "v_lshrrev_b32 v75, 8, v74\n"
    "s_mov_b32 s53, s38\n"
    "s_cmp_eq_u32 s34, 0\n"
    "s_cbranch_scc1 .Lrt_try\n"
    "s_sleep 10\n"
    "s_branch .Lrt_again\n"
    ".Lpad1:\n"
    "s_branch .Lcompute\n"
    ".Lrt_again:\n"
    "s_sub_u32 s53, s53, 1\n"
    "s_cbranch_scc1 .Lrt_ok\n"
    "buffer_load_dwordx4 v[28:31], v57, s[36:39], s52 offen sc1\n"
    "buffer_load_dwordx2 v[44:45], v58, s[36:39], s52 offen sc1\n"
    ".Lrt_try:\n"
    "s_waitcnt vmcnt(0)\n"
    "v_and_or_b32 v46, v44, 3, v55\n"
    "v_xor_b32 v40, s45, v29\n"
    "v_lshrrev_b32 v47, 1, v46\n"
    "v_xor_b32 v41, s45, v31\n"
    "v_bitop3_b32 v47, v47, v61, 7 bitop3:0x28\n"
    "v_bitop3_b32 v41, v41, v45, s45 bitop3:0xf6\n"
    "v_lshl_add_u32 v47, v47, 4, v73\n"
    "v_or_b32 v40, v40, v41\n"
    "v_lshl_add_u32 v46, v46, 7, v47\n"
    "v_cmp_ne_u32 vcc, 0, v40\n"
    "v_xor_b32 v47, 64, v46\n"
    "ds_read_b128 v[48:51], v46\n"
    "ds_read_b128 v[20:23], v47\n"
    "s_cbranch_vccnz .Lrt_again\n"
    ".Lrt_ok:\n"
    "s_barrier\n"
    "ds_read_b64_tr_b16 v[12:13], v54 offset:8192\n"
    "ds_read_b64_tr_b16 v[14:15], v54 offset:8704\n"
    "ds_read_b64_tr_b16 v[16:17], v54 offset:16384\n"
    "ds_read_b64_tr_b16 v[18:19], v54 offset:16896\n"
    "ds_write2_b32 v60, v28, v30 offset0:0 offset1:4\n"
    "ds_read_b128 v[76:79], v72 offset:6400\n"
    "v_bfe_i32 v40, v28, 0, 1\n"
    "v_bfe_i32 v41, v28, 1, 1\n"
    "v_bfe_i32 v42, v30, 0, 1\n"
    "v_bfe_i32 v43, v30, 1, 1\n"
    "v_bfi_b32 v32, v40, v33, v32\n"
    "v_bfi_b32 v34, v40, v35, v34\n"
    "v_bfi_b32 v36, v42, v37, v36\n"
    "v_bfi_b32 v38, v42, v39, v38\n"
    "v_bfi_b32 v32, v41, v34, v32\n"
    "v_bfi_b32 v33, v43, v38, v36\n"
    "s_nop 0\n"
    "s_waitcnt lgkmcnt(4)\n"
    "v_mfma_f32_16x16x32_bf16 v[32:35], v[48:51], v[12:15], v[32:35]\n"
    "s_waitcnt lgkmcnt(2)\n"
    "v_mfma_f32_16x16x32_bf16 v[32:35], v[20:23], v[16:19], v[32:35]\n"
    "s_waitcnt lgkmcnt(0)\n"
    "v_add_u16 v76, v76, v78\n"
    "v_add_u16 v77, v77, v79\n"
    "v_cmp_gt_u16 vcc, v76, v77\n"
    "s_and_b32 s14, vcc_lo, 0x2000\n"
    "s_and_b32 s12, s14, s13\n"
    "s_xor_b32 s48, s48, s12\n"
    "s_nop 0\n"
    "v_bfi_b32 v32, s51, v74, v32\n"
    "v_bfi_b32 v33, s51, v75, v33\n"
    "buffer_store_dwordx2 v[32:33], v56, s[36:39], s48 offen sc1\n"
    "s_barrier\n"
    ".Lphase2:\n"
    "s_cmp_lt_u32 s47, 4\n"
    "s_cbranch_scc1 .Lend\n"
    "s_xor_b32 s54, s54, s14\n"
    "s_xor_b32 s58, s58, s14\n"
    "s_xor_b32 s55, s55, s14\n"
    "s_add_i32 s48, s54, 16384\n"
    "s_add_i32 s49, s15, 0x1000\n"
    "s_movk_i32 s51, 0xff\n"
    "s_mov_b32 s24, 0xbfb8aa3b\n"
    "s_mov_b32 s25, s24\n"
    "s_mov_b32 s53, s38\n"
    "s_and_b32 s46, s45, s51\n"
    "s_bfe_u32 s8, s45, 0x80008\n"
    "s_bfe_u32 s9, s45, 0x80010\n"
    "s_lshr_b32 s10, s45, 24\n"
    "v_mov_b64 v[40:41], 0\n"
    "v_mov_b64 v[42:43], 0\n"
    "s_sleep 8\n"
    "s_cmp_lt_i32 s15, 0\n"
    "s_cbranch_scc1 .Lnox\n"
    "buffer_load_dwordx4 v[40:43], v64, s[36:39], s15 offen sc1\n"
    "buffer_load_dwordx4 v[36:39], v64, s[36:39], s49 offen sc1\n"
    ".Lnox:\n"
    "s_sleep 5\n"
    "buffer_load_dwordx4 v[20:23], v64, s[36:39], s54 offen sc1\n"
    "buffer_load_dwordx4 v[24:27], v64, s[36:39], s48 offen sc1\n"
    "s_waitcnt vmcnt(2) lgkmcnt(0)\n"
    "s_barrier\n"
    "ds_read_b32 v68, v68 offset:6400\n"
    "ds_read_b64_tr_b16 v[4:5], v65 offset:0\n"
    "ds_read_b64_tr_b16 v[6:7], v65 offset:1024\n"
    "ds_read_b64_tr_b16 v[8:9], v65 offset:8192\n"
    "ds_read_b64_tr_b16 v[10:11], v65 offset:9216\n"
    "ds_read_b64_tr_b16 v[12:13], v65 offset:16384\n"
    "ds_read_b64_tr_b16 v[14:15], v65 offset:17408\n"
    "ds_read_b64_tr_b16 v[16:17], v65 offset:24576\n"
    "ds_read_b64_tr_b16 v[18:19], v65 offset:25600\n"
    "s_cbranch_scc1 .Lg_try\n"
    ".Lx_try:\n"
    "v_xor_b32 v40, s46, v40\n"
    "v_xor_b32 v41, s8, v41\n"
    "v_xor_b32 v42, s9, v42\n"
    "v_xor_b32 v43, s10, v43\n"
    "v_xor_b32 v36, s46, v36\n"
    "v_xor_b32 v37, s8, v37\n"
    "v_xor_b32 v38, s9, v38\n"
    "v_xor_b32 v39, s10, v39\n"
    "v_or3_b32 v28, v40, v41, v42\n"
    "v_or3_b32 v28, v28, v43, v36\n"
    "v_or3_b32 v29, v37, v38, v39\n"
    "v_or_b32 v28, v28, v29\n"
    "v_cmp_ne_u32_sdwa vcc, v28, 0 src0_sel:BYTE_0 src1_sel:DWORD\n"
    "v_pk_add_f32 v[40:41], v[40:41], v[36:37]\n"
    "v_pk_add_f32 v[42:43], v[42:43], v[38:39]\n"
    "s_cbranch_vccz .Lg_try\n"
    "s_sub_u32 s53, s53, 1\n"
    "s_cbranch_scc1 .Lg_ok\n"
    "buffer_load_dwordx4 v[40:43], v64, s[36:39], s15 offen sc1\n"
    "buffer_load_dwordx4 v[36:39], v64, s[36:39], s49 offen sc1\n"
    "s_waitcnt vmcnt(0)\n"
    "s_branch .Lx_try\n"
    ".Lpad2:\n"
    "s_branch .Lcompute\n"
    ".Lg_again:\n"
    "s_sub_u32 s53, s53, 1\n"
    "s_cbranch_scc1 .Lg_ok\n"
    "buffer_load_dwordx4 v[20:23], v64, s[36:39], s54 offen sc1\n"
    "buffer_load_dwordx4 v[24:27], v64, s[36:39], s48 offen sc1\n"
    ".Lg_try:\n"
    "s_waitcnt vmcnt(1)\n"
    "v_xor_b32 v20, s46, v20\n"
    "v_xor_b32 v21, s8, v21\n"
    "v_xor_b32 v22, s9, v22\n"
    "v_xor_b32 v23, s10, v23\n"
    "v_or3_b32 v28, v20, v21, v22\n"
    "v_or_b32 v28, v28, v23\n"
    "v_pk_add_f32 v[20:21], v[20:21], v[40:41]\n"
    "v_pk_add_f32 v[22:23], v[22:23], v[42:43]\n"
    "s_waitcnt vmcnt(0)\n"
    "v_xor_b32 v24, s46, v24\n"
    "v_xor_b32 v25, s8, v25\n"
    "v_xor_b32 v26, s9, v26\n"
    "v_xor_b32 v27, s10, v27\n"
    "v_pk_add_f32 v[20:21], v[20:21], v[24:25]\n"
    "v_pk_add_f32 v[22:23], v[22:23], v[26:27]\n"
    "v_or3_b32 v28, v28, v24, v25\n"
    "v_permlane32_swap_b32 v20, v21\n"
    "v_or3_b32 v28, v28, v26, v27\n"
    "v_permlane32_swap_b32 v22, v23\n"
    "v_cmp_ne_u32_sdwa vcc, v28, 0 src0_sel:BYTE_0 src1_sel:DWORD\n"
    "v_cvt_pk_bf16_f32 v24, 0, v20\n"
    "v_cvt_pk_bf16_f32 v25, 0, v22\n"
    "v_pk_mul_f32 v[26:27], v[24:25], s[24:25]\n"
    "v_exp_f32 v26, v26\n"
    "v_exp_f32 v27, v27\n"
    "s_cbranch_vccnz .Lg_again\n"
    ".Lg_ok:\n"
    "v_pk_add_f32 v[26:27], v[26:27], 1.0 op_sel_hi:[1,0]\n"
    "v_rcp_f32 v26, v26\n"
    "v_rcp_f32 v27, v27\n"
    "v_cvt_pk_bf16_f32 v28, 0, v21\n"
    "v_pk_mul_f32 v[24:25], v[24:25], v[26:27]\n"
    "v_cvt_pk_bf16_f32 v29, 0, v23\n"
    "v_cvt_pk_bf16_f32 v24, 0, v24\n"
    "v_cvt_pk_bf16_f32 v25, 0, v25\n"
    "v_pk_mul_f32 v[24:25], v[24:25], v[28:29]\n"
    "v_cvt_pk_bf16_f32 v24, v24, v25\n"
    "ds_write_b32 v66, v24 offset:4096\n"
    "s_waitcnt lgkmcnt(0)\n"
    "s_barrier\n"
    "ds_read_b128 v[20:23], v67 offset:4096\n"
    "ds_read_b128 v[24:27], v67 offset:4160\n"
    "ds_read_b128 v[28:31], v67 offset:4224\n"
    "ds_read_b128 v[32:35], v67 offset:4288\n"
    "s_mov_b32 s53, s38\n"
    "s_cmp_lg_u32 s57, 0\n"
    "s_waitcnt lgkmcnt(3)\n"
    "v_mfma_f32_16x16x32_bf16 v[36:39], v[20:23], v[4:7], 0\n"
    "s_waitcnt lgkmcnt(2)\n"
    "v_mfma_f32_16x16x32_bf16 v[36:39], v[24:27], v[8:11], v[36:39]\n"
    "s_waitcnt lgkmcnt(1)\n"
    "v_mfma_f32_16x16x32_bf16 v[36:39], v[28:31], v[12:15], v[36:39]\n"
    "s_waitcnt lgkmcnt(0)\n"
    "v_mfma_f32_16x16x32_bf16 v[36:39], v[32:35], v[16:19], v[36:39]\n"
    "s_nop 7\n"
    "v_bfi_b32 v36, s51, v74, v36\n"
    "v_bfi_b32 v37, s51, v75, v37\n"
    "buffer_store_dwordx2 v[36:37], v70, s[36:39], s58 offen sc1\n"
    "s_cbranch_scc1 .Lend\n"
    "v_and_b32 v40, 3, v68\n"
    "v_lshl_or_b32 v69, v40, 11, v69\n"
    "buffer_load_ushort v46, v69, s[20:23], s56 offen\n"
    "v_and_b32 v44, 0xffff0000, v68\n"
    "v_mov_b64 v[20:21], 0\n"
    "v_mov_b64 v[22:23], 0\n"
    "v_mov_b32 v48, s45\n"
    "s_mov_b32 exec_hi, 0xffffff\n"
    "s_mov_b32 s50, 229376\n"
    "s_add_i32 s48, s55, s50\n"
    "s_add_i32 s49, s48, s50\n"
    "s_add_i32 s50, s49, s50\n"
    "s_sleep 16\n"
    ".Lp_try:\n"
    "buffer_load_dwordx4 v[20:23], v71, s[36:39], s55 offen sc1\n"
    "buffer_load_dwordx4 v[4:7], v71, s[36:39], s48 offen sc1\n"
    "buffer_load_dwordx4 v[8:11], v71, s[36:39], s49 offen sc1\n"
    "buffer_load_dwordx4 v[12:15], v71, s[36:39], s50 offen sc1\n"
    "s_waitcnt vmcnt(4)\n"
    "v_lshlrev_b32 v45, 16, v46\n"
    "v_fmac_f32 v45, 0, v45\n"
    "s_sub_u32 s53, s53, 1\n"
    "s_waitcnt vmcnt(3)\n"
    "v_xor_b32 v20, s46, v20\n"
    "v_xor_b32 v21, s8, v21\n"
    "v_xor_b32 v22, s9, v22\n"
    "v_xor_b32 v23, s10, v23\n"
    "v_or3_b32 v28, v20, v21, v22\n"
    "v_or_b32 v28, v28, v23\n"
    "s_waitcnt vmcnt(2)\n"
    "v_xor_b32 v4, s46, v4\n"
    "v_xor_b32 v5, s8, v5\n"
    "v_xor_b32 v6, s9, v6\n"
    "v_xor_b32 v7, s10, v7\n"
    "v_or3_b32 v28, v28, v4, v5\n"
    "v_or3_b32 v28, v28, v6, v7\n"
    "v_pk_add_f32 v[20:21], v[20:21], v[4:5]\n"
    "v_pk_add_f32 v[22:23], v[22:23], v[6:7]\n"
    "s_waitcnt vmcnt(1)\n"
    "v_xor_b32 v8, s46, v8\n"
    "v_xor_b32 v9, s8, v9\n"
    "v_xor_b32 v10, s9, v10\n"
    "v_xor_b32 v11, s10, v11\n"
    "v_or3_b32 v28, v28, v8, v9\n"
    "v_or3_b32 v28, v28, v10, v11\n"
    "v_pk_add_f32 v[20:21], v[20:21], v[8:9]\n"
    "v_pk_add_f32 v[22:23], v[22:23], v[10:11]\n"
    "s_waitcnt vmcnt(0)\n"
    "v_xor_b32 v12, s46, v12\n"
    "v_xor_b32 v13, s8, v13\n"
    "v_xor_b32 v14, s9, v14\n"
    "v_xor_b32 v15, s10, v15\n"
    "v_or3_b32 v28, v28, v12, v13\n"
    "v_or3_b32 v28, v28, v14, v15\n"
    "v_cmp_ne_u32_sdwa vcc, v28, 0 src0_sel:BYTE_0 src1_sel:DWORD\n"
    "v_pk_add_f32 v[20:21], v[20:21], v[12:13]\n"
    "v_pk_add_f32 v[22:23], v[22:23], v[14:15]\n"
    "s_cbranch_scc1 .Lp_ok\n"
    "s_cbranch_vccnz .Lp_try\n"
    ".Lp_ok:\n"
    "s_mov_b32 exec_hi, -1\n"
    "v_permlane32_swap_b32 v20, v21\n"
    "v_permlane32_swap_b32 v22, v23\n"
    "v_add_f32 v20, v20, v21\n"
    "v_add_f32 v22, v22, v23\n"
    "s_or_b32 s48, s19, s17\n"
    "s_nop 0\n"
    "v_permlane16_swap_b32 v20, v22\n"
    "v_add_f32 v20, v20, v22\n"
    "s_nop 1\n"
    "v_add_f32_dpp v20, v20, v20 row_ror:8 row_mask:0xf bank_mask:0xf\n"
    "s_mov_b32 exec_lo, 0x00ff00ff\n"
    "v_cvt_pk_bf16_f32 v20, 0, v20\n"
    "s_mov_b32 exec_hi, exec_lo\n"
    "v_mul_f32 v20, v20, v44\n"
    "v_cvt_pk_bf16_f32 v20, 0, v20\n"
    "v_add_f32 v20, v45, v20\n"
    "v_cvt_pk_bf16_f32 v20, v20, v20\n"
    "buffer_store_short v20, v69, s[40:43], s56 offen\n"
    "s_cbranch_scc1 .Lend\n"
    "s_mov_b64 exec, 1\n"
    "buffer_store_dword v48, off, s[36:39], 0 sc1\n"
    ".Lend:\n"
    "s_endpgm\n"
    ".p2align 12\n"
    :
    : [ka] "s"(__builtin_amdgcn_kernarg_segment_ptr()), [bid] "s"(blockIdx.x), [tid] "v"(threadIdx.x),
      [lds] "i"((__attribute__((address_space(3))) char*)lds_)
    : "memory", "scc", "vcc", "s3", "s4", "s5", "s6", "s7", "s8", "s9", "s10", "s11", "s12", "s13", "s14", "s15",
      "s16", "s17", "s18", "s19", "s20", "s21", "s22", "s23", "s24", "s25", "s26", "s27", "s28", "s29", "s30", "s31",
      "s34", "s35", "s36", "s37", "s38", "s39", "s40", "s41", "s42", "s43", "s44", "s45", "s46", "s47", "s48", "s49",
      "s50", "s51", "s52", "s53", "s54", "s55", "s56", "s57", "s58", "s59", "v1", "v2", "v3", "v4", "v5", "v6",
      "v7", "v8", "v9", "v10", "v11", "v12", "v13", "v14", "v15", "v16", "v17", "v18", "v19", "v20", "v21", "v22",
      "v23", "v24", "v25", "v26", "v27", "v28", "v29", "v30", "v31", "v32", "v33", "v34", "v35", "v36", "v37", "v38",
      "v39", "v40", "v41", "v42", "v43", "v44", "v45", "v46", "v47", "v48", "v49", "v50", "v51", "v52", "v53", "v54",
      "v55", "v56", "v57", "v58", "v59", "v60", "v61", "v62", "v63", "v64", "v65", "v66", "v67", "v68", "v69", "v70",
      "v71", "v72", "v73", "v74", "v75", "v76", "v77", "v78", "v79");
}
}

extern "C" void kernel_launch(void* const* d_in, const int* in_sizes, int n_in, void* d_out, int out_size, void* d_ws, size_t ws_size, hipStream_t stream) {
  mod_ffn<<<256, 512, 0, stream>>>(d_in[0], d_in[1], d_in[2], d_in[3], d_in[4], d_out, d_ws);
}
```
